```python
import torch

def _softmax(x, axis=-1):
    return torch.softmax(x, dim=axis)

def _gelu(x):
    return torch.nn.functional.gelu(x, approximate='tanh')

def _silu(x):
    return torch.nn.functional.silu(x)

def _sigmoid(x):
    return torch.sigmoid(x)

def _layernorm(x, axis=-1, eps=1e-05):
    m = x.mean(dim=axis, keepdim=True)
    v = x.var(dim=axis, keepdim=True, unbiased=False)
    return (x - m) / torch.sqrt(v + eps)

def _scatter_add(tgt, idx, src, axis=0):
    return tgt.clone().index_add_(axis, idx, src)

def _segment_sum(src, seg, n):
    out = torch.zeros((n,) + src.shape[1:], dtype=src.dtype)
    return out.index_add_(0, seg, src)

def _pad(x, pw):
    flat = tuple((v for pair in reversed(pw) for v in pair))
    return torch.nn.functional.pad(x, flat)
kernel_name = 'adaLN_cond__memory_aug_recurrent_mem_tr__glu_gated_reglu__prompt'

def setup_inputs():

    _w = lambda a: (lambda t: t.to(torch.bfloat16) if t.is_floating_point() else t)(torch.as_tensor(a))

    def _A_setup():
        B, S, Dm = (1, 2048, 576)
        H, Dh = (32, 128)
        M, seg = (8, 32)
        seg = min(seg, S)
        S = S // seg * seg
        return {'x': _w((lambda *s: torch.randn(*s, dtype=torch.bfloat16) * 0.05)(B, S, Dm)), 'mem0': _w((lambda *s: torch.randn(*s, dtype=torch.bfloat16) * 0.8)(B, M, Dm)), 'Wq': _w((lambda *s: torch.randn(*s, dtype=torch.bfloat16) * 0.8)(Dm, H, Dh) * Dm ** (-0.5)), 'Wk': _w((lambda *s: torch.randn(*s, dtype=torch.bfloat16) * 0.8)(Dm, H, Dh) * Dm ** (-0.5)), 'Wv': _w((lambda *s: torch.randn(*s, dtype=torch.bfloat16) * 0.8)(Dm, H, Dh) * Dm ** (-0.5)), 'Wo': _w((lambda *s: torch.randn(*s, dtype=torch.bfloat16) * 0.8)(H, Dh, Dm) * (H * Dh) ** (-0.5)), 'seg_len': seg}
    _A_in = _A_setup()

    def _B_setup():
        B, S, D, F = (1, 2048, 576, 4608)
        return {'x': _w((lambda *s: torch.randn(*s, dtype=torch.bfloat16) * 0.05)(B, S, D)), 'W_gate': _w((lambda *s: torch.randn(*s, dtype=torch.bfloat16) * 0.4)(D, F) * (2.0 / D) ** 0.5), 'W_up': _w((lambda *s: torch.randn(*s, dtype=torch.bfloat16) * 0.4)(D, F) * (2.0 / D) ** 0.5), 'W_down': _w((lambda *s: torch.randn(*s, dtype=torch.bfloat16) * 0.4)(F, D) * (1.0 / F) ** 0.5)}
    _B_in = _B_setup()
    return {'A_x': _A_in['x'], 'A_mem0': _A_in['mem0'], 'A_Wq': _A_in['Wq'], 'A_Wk': _A_in['Wk'], 'A_Wv': _A_in['Wv'], 'A_Wo': _A_in['Wo'], 'A_seg_len': _A_in['seg_len'], 'B_x': _B_in['x'], 'B_W_gate': _B_in['W_gate'], 'B_W_up': _B_in['W_up'], 'B_W_down': _B_in['W_down'], 'c': _w((lambda *s: torch.randn(*s, dtype=torch.bfloat16) * 0.1)(1, 576) * 8.0), 'w_mod': _w((lambda *s: torch.randn(*s, dtype=torch.bfloat16) * 0.1)(576, 6 * 576) * 576 ** (-0.5) * 8.0)}

def reference(A_x, A_mem0, A_Wq, A_Wk, A_Wv, A_Wo, A_seg_len, B_x, B_W_gate, B_W_up, B_W_down, c, w_mod):

    def _A_ref(x, mem0, Wq, Wk, Wv, Wo, seg_len):
        B, S, Dm = x.shape
        H, Dh = (Wq.shape[1], Wq.shape[2])
        M = mem0.shape[1]
        scale = Dh ** (-0.5)
        n_seg = S // seg_len
        L = M + seg_len
        idx = torch.arange(L)
        mask = (idx[:, None] < M) | (idx[:, None] >= idx[None, :])
        mem = mem0
        outs = []
        for s in range(n_seg):
            xs = x[:, s * seg_len:(s + 1) * seg_len, :].clone()
            h = torch.concatenate([mem, xs], axis=1)
            q = torch.einsum('btd,dhe->bhte', h, Wq)
            k = torch.einsum('btd,dhe->bhte', h, Wk)
            v = torch.einsum('btd,dhe->bhte', h, Wv)
            scores = torch.einsum('bhqe,bhke->bhqk', q, k) * scale
            scores = torch.where(mask[None, None], scores, -1000000000.0)
            p = _softmax(scores, axis=-1)
            o = torch.einsum('bhqk,bhke->bhqe', p, v)
            o = torch.einsum('bhte,hed->btd', o, Wo)
            mem = o[:, :M, :].clone()
            outs.append(o[:, M:, :])
        return torch.concatenate(outs, axis=1)

    def _B_ref(x, W_gate, W_up, W_down):
        gate = torch.einsum('bsd,df->bsf', x, W_gate)
        up = torch.einsum('bsd,df->bsf', x, W_up)
        gate = gate * (lambda x, dt: x.to(dt))(gate > 0, gate.dtype)
        h = gate * up
        return torch.einsum('bsf,fd->bsd', h, W_down)
    x = A_x
    mod = (_silu(c) @ w_mod).reshape(c.shape[0], 6, 1, -1)
    sa, ca, ga, sb, cb, gb = (mod[:, 0].clone(), mod[:, 1].clone(), mod[:, 2].clone(), mod[:, 3].clone(), mod[:, 4].clone(), mod[:, 5].clone())
    h = _layernorm(x, axis=-1) * (1 + ca) + sa
    x = x + ga * _A_ref(h, A_mem0, A_Wq, A_Wk, A_Wv, A_Wo, A_seg_len)
    h = _layernorm(x, axis=-1) * (1 + cb) + sb
    x = x + gb * _B_ref(h, B_W_gate, B_W_up, B_W_down)
    return x
```

```cpp
#include <hip/hip_runtime.h>
#include <hip/hip_bf16.h>
#include <stdint.h>

typedef unsigned short u16;
typedef short bf16x8 __attribute__((ext_vector_type(8)));
typedef short bf16x4 __attribute__((ext_vector_type(4)));
typedef float f32x4 __attribute__((ext_vector_type(4)));
typedef __attribute__((address_space(3))) bf16x4 lds_bf16x4;

constexpr int S_ = 2048, D_ = 576, H_ = 32, E_ = 128, HE_ = 4096, F_ = 4608, MM_ = 8, SEG_ = 32, NSEG_ = 64;
constexpr int QKV_ = 3 * HE_;

__device__ __forceinline__ float bf2f(u16 v) { return __uint_as_float(((unsigned)v) << 16); }
__device__ __forceinline__ u16 f2bf(float f) {
  unsigned u = __float_as_uint(f);
  u += 0x7fffu + ((u >> 16) & 1u);
  return (u16)(u >> 16);
}
__device__ __forceinline__ f32x4 mfma16(bf16x8 a, bf16x8 b, f32x4 c) {
  return __builtin_amdgcn_mfma_f32_16x16x32_bf16(a, b, c, 0, 0, 0);
}
__device__ __forceinline__ bf16x8 tr_frag(const u16* lds_tile, int stride, int k0, int n0, int lane) {
  int g = lane >> 4, q = (lane & 15) >> 2, p = lane & 3;
  const u16* a0 = lds_tile + (k0 + 8 * g + q) * stride + n0 + 4 * p;
  bf16x4 r0 = __builtin_amdgcn_ds_read_tr16_b64_v4i16((lds_bf16x4*)a0);
  bf16x4 r1 = __builtin_amdgcn_ds_read_tr16_b64_v4i16((lds_bf16x4*)(a0 + 4 * stride));
  bf16x8 r;
  r[0] = r0[0]; r[1] = r0[1]; r[2] = r0[2]; r[3] = r0[3];
  r[4] = r1[0]; r[5] = r1[1]; r[6] = r1[2]; r[7] = r1[3];
  return r;
}

__global__ __launch_bounds__(256) void k_mod(const u16* __restrict__ c, const u16* __restrict__ w, float* __restrict__ mod) {
  __shared__ float sc[D_];
  __shared__ float red[4][64];
  for (int i = threadIdx.x; i < D_; i += 256) {
    float v = bf2f(c[i]);
    float s = v / (1.f + __expf(-v));
    sc[i] = bf2f(f2bf(s));
  }
  __syncthreads();
  int n = blockIdx.x * 64 + (threadIdx.x & 63);
  int part = threadIdx.x >> 6;
  float acc = 0.f;
  for (int k = part * 144; k < part * 144 + 144; ++k) acc += sc[k] * bf2f(w[(size_t)k * (6 * D_) + n]);
  red[part][threadIdx.x & 63] = acc;
  __syncthreads();
  if (part == 0) {
    float a = red[0][threadIdx.x] + red[1][threadIdx.x] + red[2][threadIdx.x] + red[3][threadIdx.x];
    mod[n] = bf2f(f2bf(a));
  }
}

template <bool F32IN>
__global__ __launch_bounds__(256) void k_ln(const void* __restrict__ xin, const float* __restrict__ sh, const float* __restrict__ scl,
                                            u16* __restrict__ out) {
  int row = blockIdx.x * 4 + (threadIdx.x >> 6);
  int lane = threadIdx.x & 63;
  float v[9];
  float s = 0.f;
#pragma unroll
  for (int i = 0; i < 9; ++i) {
    int c = lane + 64 * i;
    v[i] = F32IN ? ((const float*)xin)[(size_t)row * D_ + c] : bf2f(((const u16*)xin)[(size_t)row * D_ + c]);
    s += v[i];
  }
#pragma unroll
  for (int o = 32; o > 0; o >>= 1) s += __shfl_xor(s, o);
  float m = s * (1.f / D_);
  float q = 0.f;
#pragma unroll
  for (int i = 0; i < 9; ++i) { float d = v[i] - m; q += d * d; }
#pragma unroll
  for (int o = 32; o > 0; o >>= 1) q += __shfl_xor(q, o);
  float rs = rsqrtf(q * (1.f / D_) + 1e-5f);
#pragma unroll
  for (int i = 0; i < 9; ++i) {
    int c = lane + 64 * i;
    out[(size_t)row * D_ + c] = f2bf((v[i] - m) * rs * (1.f + scl[c]) + sh[c]);
  }
}

constexpr int AST = 72, BST = 136;
template <int MODE>
__device__ __forceinline__ const u16* bsrc(const u16* Bp, const u16* B0, const u16* B1, int ldb, int n0, int r, int c) {
  if (MODE == 2) {
    int w = c >> 6, rr = c & 63;
    return (rr < 32 ? B0 : B1) + (size_t)r * ldb + n0 + 32 * w + (rr & 31);
  } else if (MODE == 0) {
    return Bp + (size_t)r * ldb + n0 + c;
  } else {
    int cc = n0 + c; if (cc > D_ - 8) cc = D_ - 8;
    return Bp + (size_t)r * ldb + cc;
  }
}
template <int MODE>
__global__ __launch_bounds__(256) void k_gemm(const u16* __restrict__ A, int K, const u16* __restrict__ B0, const u16* __restrict__ B1,
                                              const u16* __restrict__ B2, void* __restrict__ Cout, const void* __restrict__ res,
                                              const float* __restrict__ gvec) {
  __shared__ __attribute__((aligned(16))) u16 sA[128 * AST];
  __shared__ __attribute__((aligned(16))) u16 sB[64 * BST];
  const int tid = threadIdx.x, lane = tid & 63, wid = tid >> 6, wm = wid >> 1, wn = wid & 1;
  const int m0 = blockIdx.y * 128;
  const int nb = blockIdx.x;
  int ldb;
  const u16* Bp;
  int n0;
  if (MODE == 0) { int n = nb * 128; int which = n / HE_; Bp = which == 0 ? B0 : (which == 1 ? B1 : B2); n0 = n - which * HE_; ldb = HE_; }
  else if (MODE == 2) { n0 = nb * 64; ldb = F_; Bp = B0; }
  else { n0 = nb * 128; ldb = D_; Bp = B0; }
  f32x4 acc[4][4];
#pragma unroll
  for (int i = 0; i < 4; ++i)
#pragma unroll
    for (int j = 0; j < 4; ++j) acc[i][j] = (f32x4){0.f, 0.f, 0.f, 0.f};
  uint4 ra0, ra1, ra2, ra3, rb0, rb1, rb2, rb3;
#define GL_A(i, k0) (*(const uint4*)(A + (size_t)(m0 + ((tid + 256 * i) >> 3)) * K + (k0) + ((tid + 256 * i) & 7) * 8))
#define GL_B(i, k0) (*(const uint4*)bsrc<MODE>(Bp, B0, B1, ldb, n0, ((tid + 256 * i) >> 4) + (k0), ((tid + 256 * i) & 15) * 8))
#define gload(k0) do { ra0 = GL_A(0, k0); ra1 = GL_A(1, k0); ra2 = GL_A(2, k0); ra3 = GL_A(3, k0); \
    rb0 = GL_B(0, k0); rb1 = GL_B(1, k0); rb2 = GL_B(2, k0); rb3 = GL_B(3, k0); } while (0)
  gload(0);
  for (int k0 = 0; k0 < K; k0 += 64) {
#define ST_AB(i, va, vb) *(uint4*)(sA + ((tid + 256 * i) >> 3) * AST + ((tid + 256 * i) & 7) * 8) = va; \
    *(uint4*)(sB + ((tid + 256 * i) >> 4) * BST + ((tid + 256 * i) & 15) * 8) = vb;
    ST_AB(0, ra0, rb0) ST_AB(1, ra1, rb1) ST_AB(2, ra2, rb2) ST_AB(3, ra3, rb3)
    __syncthreads();
    if (k0 + 64 < K) gload(k0 + 64);
#pragma unroll
    for (int ks = 0; ks < 2; ++ks) {
      bf16x8 af[4], bfr[4];
#pragma unroll
      for (int i = 0; i < 4; ++i) af[i] = *(const bf16x8*)(sA + (wm * 64 + i * 16 + (lane & 15)) * AST + ks * 32 + (lane >> 4) * 8);
#pragma unroll
      for (int j = 0; j < 4; ++j) bfr[j] = tr_frag(sB, BST, ks * 32, wn * 64 + j * 16, lane);
#pragma unroll
      for (int i = 0; i < 4; ++i)
#pragma unroll
        for (int j = 0; j < 4; ++j) acc[i][j] = mfma16(af[i], bfr[j], acc[i][j]);
    }
    __syncthreads();
  }
#pragma unroll
  for (int i = 0; i < 4; ++i) {
#pragma unroll
    for (int r = 0; r < 4; ++r) {
      int row = m0 + wm * 64 + i * 16 + (lane >> 4) * 4 + r;
      if (MODE == 2) {
#pragma unroll
        for (int j = 0; j < 2; ++j) {
          int col = n0 + 32 * wn + j * 16 + (lane & 15);
          float g = bf2f(f2bf(acc[i][j][r])), u = bf2f(f2bf(acc[i][j + 2][r]));
          float t = g > 0.f ? g * u : 0.f;
          ((u16*)Cout)[(size_t)row * F_ + col] = f2bf(t);
        }
      } else {
#pragma unroll
        for (int j = 0; j < 4; ++j) {
          int col = nb * 128 + wn * 64 + j * 16 + (lane & 15);
          if (MODE == 0) {
            ((u16*)Cout)[(size_t)row * QKV_ + col] = f2bf(acc[i][j][r]);
          } else if (col < D_) {
            float a = bf2f(f2bf(acc[i][j][r]));
            if (MODE == 1) {
              float xv = bf2f(((const u16*)res)[(size_t)row * D_ + col]);
              ((float*)Cout)[(size_t)row * D_ + col] = xv + gvec[col] * a;
            } else {
              float xv = ((const float*)res)[(size_t)row * D_ + col];
              ((u16*)Cout)[(size_t)row * D_ + col] = f2bf(xv + gvec[col] * a);
            }
          }
        }
      }
    }
  }
}

constexpr int VST = 136;
constexpr int PST = 72;
__global__ __launch_bounds__(256) void k_attn_x(const u16* __restrict__ qkv, const u16* __restrict__ kmem, const u16* __restrict__ vmem,
                                                u16* __restrict__ ox) {
  __shared__ __attribute__((aligned(16))) u16 sV[4][64 * VST];
  __shared__ __attribute__((aligned(16))) u16 sP[4][32 * PST];
  const int lane = threadIdx.x & 63, wid = threadIdx.x >> 6;
  const int item = blockIdx.x * 4 + wid;
  const int s = item >> 5, h = item & 31;
  u16* V = sV[wid];
  u16* P = sP[wid];
  const u16* qbase = qkv + (size_t)(s * SEG_) * QKV_ + h * E_;
  const u16* kx = qbase + HE_;
  const u16* vx = qbase + 2 * HE_;
  const u16* km = kmem + ((size_t)s * MM_) * HE_ + h * E_;
  const u16* vm = vmem + ((size_t)s * MM_) * HE_ + h * E_;
#pragma unroll
  for (int i = 0; i < 16; ++i) {
    int ch = lane + 64 * i, r = ch >> 4, c = (ch & 15) * 8;
    uint4 v = make_uint4(0, 0, 0, 0);
    if (r < 8) v = *(const uint4*)(vm + (size_t)r * HE_ + c);
    else if (r < 40) v = *(const uint4*)(vx + (size_t)(r - 8) * QKV_ + c);
    *(uint4*)(V + r * VST + c) = v;
  }
  const int g = lane >> 4, l15 = lane & 15;
  f32x4 sc[2][3];
#pragma unroll
  for (int i = 0; i < 2; ++i)
#pragma unroll
    for (int j = 0; j < 3; ++j) sc[i][j] = (f32x4){0.f, 0.f, 0.f, 0.f};
#pragma unroll
  for (int ks = 0; ks < 4; ++ks) {
    bf16x8 a[2], b[3];
#pragma unroll
    for (int i = 0; i < 2; ++i) a[i] = *(const bf16x8*)(qbase + (size_t)(i * 16 + l15) * QKV_ + ks * 32 + g * 8);
    b[0] = *(const bf16x8*)(km + (size_t)(l15 & 7) * HE_ + ks * 32 + g * 8);
    b[1] = *(const bf16x8*)(kx + (size_t)l15 * QKV_ + ks * 32 + g * 8);
    b[2] = *(const bf16x8*)(kx + (size_t)(16 + l15) * QKV_ + ks * 32 + g * 8);
#pragma unroll
    for (int i = 0; i < 2; ++i)
#pragma unroll
      for (int j = 0; j < 3; ++j) sc[i][j] = mfma16(a[i], b[j], sc[i][j]);
  }
  const float scale = 0.08838834764831845f;
#pragma unroll
  for (int i = 0; i < 2; ++i) {
#pragma unroll
    for (int r = 0; r < 4; ++r) {
      int qi = i * 16 + g * 4 + r;
      float v0 = (l15 < 8) ? sc[i][0][r] * scale : -1e30f;
      float v1 = (l15 <= qi) ? sc[i][1][r] * scale : -1e30f;
      float v2 = (16 + l15 <= qi) ? sc[i][2][r] * scale : -1e30f;
      float mx = fmaxf(v0, fmaxf(v1, v2));
#pragma unroll
      for (int o = 1; o < 16; o <<= 1) mx = fmaxf(mx, __shfl_xor(mx, o));
      float e0 = (l15 < 8) ? __expf(v0 - mx) : 0.f;
      float e1 = (l15 <= qi) ? __expf(v1 - mx) : 0.f;
      float e2 = (16 + l15 <= qi) ? __expf(v2 - mx) : 0.f;
      float sm = e0 + e1 + e2;
#pragma unroll
      for (int o = 1; o < 16; o <<= 1) sm += __shfl_xor(sm, o);
      float inv = 1.f / sm;
      if (l15 < 8) P[qi * PST + l15] = f2bf(e0 * inv);
      else P[qi * PST + 32 + l15] = 0;
      P[qi * PST + 8 + l15] = f2bf(e1 * inv);
      P[qi * PST + 24 + l15] = f2bf(e2 * inv);
      P[qi * PST + 48 + l15] = 0;
    }
  }
  __syncthreads();
#pragma unroll
  for (int i = 0; i < 2; ++i) {
    bf16x8 a0 = *(const bf16x8*)(P + (i * 16 + l15) * PST + g * 8);
    bf16x8 a1 = *(const bf16x8*)(P + (i * 16 + l15) * PST + 32 + g * 8);
#pragma unroll
    for (int j = 0; j < 8; ++j) {
      f32x4 o = (f32x4){0.f, 0.f, 0.f, 0.f};
      o = mfma16(a0, tr_frag(V, VST, 0, j * 16, lane), o);
      o = mfma16(a1, tr_frag(V, VST, 32, j * 16, lane), o);
#pragma unroll
      for (int r = 0; r < 4; ++r)
        ox[(size_t)(s * SEG_ + i * 16 + g * 4 + r) * HE_ + h * E_ + j * 16 + l15] = f2bf(o[r]);
    }
  }
}

typedef unsigned long long u64;
#define SPIN_MAX (1 << 22)
__device__ __forceinline__ void st_gran(u64* p, unsigned tag, unsigned v) {
  __hip_atomic_store(p, ((u64)tag << 32) | v, __ATOMIC_RELAXED, __HIP_MEMORY_SCOPE_AGENT);
}
__device__ __forceinline__ unsigned ld_gran(const u64* p, unsigned tag) {
  for (int it = 0; it < SPIN_MAX; ++it) {
    u64 x = __hip_atomic_load(p, __ATOMIC_RELAXED, __HIP_MEMORY_SCOPE_AGENT);
    if ((unsigned)(x >> 32) == tag) return (unsigned)x;
    __builtin_amdgcn_s_sleep(1);
  }
  return 0u;
}
constexpr int MST = 584;
struct ChainBufs {
  u64* memg;
  u64* pg;
  u64* partg;
};

__global__ __launch_bounds__(512) void k_chain(const u16* __restrict__ mem0, const u16* __restrict__ Wq, const u16* __restrict__ Wk,
                                               const u16* __restrict__ Wv, const u16* __restrict__ Wo, const u16* __restrict__ qkv,
                                               u16* __restrict__ kmem, u16* __restrict__ vmem, u64* memg, u64* pg, u64* partg) {
  __shared__ __attribute__((aligned(16))) u16 sMem[16 * MST];
  __shared__ __attribute__((aligned(16))) u16 sQ[16 * 136];
  __shared__ __attribute__((aligned(16))) u16 sK[16 * 136];
  __shared__ __attribute__((aligned(16))) u16 sV[64 * VST];
  __shared__ __attribute__((aligned(16))) u16 sP[16 * PST];
  __shared__ __attribute__((aligned(16))) u16 sStage[32 * MST];
  __shared__ float sS[8 * 48];
  __shared__ float sRed[8][144];
  const int tid = threadIdx.x, lane = tid & 63, wv = tid >> 6;
  const int g = lane >> 4, l15 = lane & 15;
  const int h = blockIdx.x >> 1;
  const bool roleA = (blockIdx.x & 1) == 0;
  for (int i = tid; i < 16 * MST; i += 512) sMem[i] = 0;
  for (int i = tid; i < 16 * 136; i += 512) { sQ[i] = 0; sK[i] = 0; }
  for (int i = tid; i < 64 * VST; i += 512) sV[i] = 0;
  for (int i = tid; i < 16 * PST; i += 512) sP[i] = 0;
  bf16x8 w1[18], w2[20];
  {
    u16* stg = sStage;
    const u16* Wa = roleA ? Wq : Wv;
#pragma unroll
    for (int ks = 0; ks < 18; ++ks) {
      __syncthreads();
      { int r = tid >> 4, c = (tid & 15) * 8;
        *(uint4*)(stg + r * 136 + c) = *(const uint4*)(Wa + (size_t)(ks * 32 + r) * HE_ + h * E_ + c); }
      __syncthreads();
      w1[ks] = tr_frag(stg, 136, 0, wv * 16, lane);
    }
    if (roleA) {
#pragma unroll
      for (int ks = 0; ks < 18; ++ks) {
        __syncthreads();
        { int r = tid >> 4, c = (tid & 15) * 8;
          *(uint4*)(stg + r * 136 + c) = *(const uint4*)(Wk + (size_t)(ks * 32 + r) * HE_ + h * E_ + c); }
        __syncthreads();
        w2[ks] = tr_frag(stg, 136, 0, wv * 16, lane);
      }
    } else {
#pragma unroll
      for (int ks = 0; ks < 4; ++ks) {
        __syncthreads();
        for (int i = tid; i < 32 * 72; i += 512) {
          int r = i / 72, c = (i % 72) * 8;
          *(uint4*)(stg + r * MST + c) = *(const uint4*)(Wo + (size_t)(h * E_ + ks * 32 + r) * D_ + c);
        }
        __syncthreads();
#pragma unroll
        for (int t = 0; t < 5; ++t) {
          int nt = wv + 8 * t; if (nt > 35) nt = 35;
          w2[t * 4 + ks] = tr_frag(stg, MST, 0, nt * 16, lane);
        }
      }
    }
  }
  __syncthreads();
  for (int s = 0; s < NSEG_; ++s) {
    const unsigned tag = s + 1;
    const int par = s & 1;
    const u16* xb = qkv + (size_t)(s * SEG_) * QKV_ + h * E_ + (roleA ? HE_ : 2 * HE_);
    {
      int r = tid >> 4, c = (tid & 15) * 8;
      uint4 v = *(const uint4*)(xb + (size_t)r * QKV_ + c);
      *(uint4*)(sV + (8 + r) * VST + c) = v;
    }
    if (s == 0) {
      for (int i = tid; i < 8 * 288; i += 512) {
        int r = i / 288, c = (i % 288) * 2;
        *(unsigned*)(sMem + r * MST + c) = *(const unsigned*)(mem0 + r * D_ + c);
      }
    } else {
      const u64* mg = memg + (size_t)par * 2304;
      for (int i = tid; i < 2304; i += 512) {
        int j = i / 72, rem = i % 72, r = rem / 9, pp = rem % 9;
        unsigned v = ld_gran(mg + i, tag);
        *(unsigned*)(sMem + r * MST + j * 18 + pp * 2) = v;
      }
    }
    __syncthreads();
    f32x4 a1 = (f32x4){0.f, 0.f, 0.f, 0.f}, a2 = (f32x4){0.f, 0.f, 0.f, 0.f};
    if (roleA) {
#pragma unroll
      for (int ks = 0; ks < 18; ++ks) {
        bf16x8 a = *(const bf16x8*)(sMem + l15 * MST + ks * 32 + g * 8);
        a1 = mfma16(a, w1[ks], a1);
        a2 = mfma16(a, w2[ks], a2);
      }
      if (g < 2) {
#pragma unroll
        for (int r = 0; r < 4; ++r) {
          int row = g * 4 + r;
          sQ[row * 136 + wv * 16 + l15] = f2bf(a1[r]);
          u16 kb = f2bf(a2[r]);
          sK[row * 136 + wv * 16 + l15] = kb;
          kmem[((size_t)s * MM_ + row) * HE_ + h * E_ + wv * 16 + l15] = kb;
        }
      }
      __syncthreads();
      if (wv < 3) {
        f32x4 sc = (f32x4){0.f, 0.f, 0.f, 0.f};
        const u16* kb = wv == 0 ? (sK + l15 * 136) : (sV + (8 + (wv - 1) * 16 + l15) * VST);
#pragma unroll
        for (int ks = 0; ks < 4; ++ks) {
          bf16x8 a = *(const bf16x8*)(sQ + l15 * 136 + ks * 32 + g * 8);
          bf16x8 b = *(const bf16x8*)(kb + ks * 32 + g * 8);
          sc = mfma16(a, b, sc);
        }
        if (g < 2) {
#pragma unroll
          for (int r = 0; r < 4; ++r) {
            int col = wv == 0 ? l15 : 8 + (wv - 1) * 16 + l15;
            if (wv != 0 || l15 < 8) sS[(g * 4 + r) * 48 + col] = sc[r] * 0.08838834764831845f;
          }
        }
      }
      __syncthreads();
      if (wv == 0) {
        int row = lane >> 3, c0 = (lane & 7) * 5;
        float v[5], mx = -1e30f;
#pragma unroll
        for (int i = 0; i < 5; ++i) { v[i] = sS[row * 48 + c0 + i]; mx = fmaxf(mx, v[i]); }
#pragma unroll
        for (int o = 1; o < 8; o <<= 1) mx = fmaxf(mx, __shfl_xor(mx, o));
        float sm = 0.f;
#pragma unroll
        for (int i = 0; i < 5; ++i) { v[i] = __expf(v[i] - mx); sm += v[i]; }
#pragma unroll
        for (int o = 1; o < 8; o <<= 1) sm += __shfl_xor(sm, o);
        float inv = 1.f / sm;
        u64* dst = pg + ((size_t)par * 32 + h) * 320 + row * 40 + c0;
#pragma unroll
        for (int i = 0; i < 5; ++i) st_gran(dst + i, tag, (unsigned)f2bf(v[i] * inv));
      }
      __syncthreads();
    } else {
#pragma unroll
      for (int ks = 0; ks < 18; ++ks) {
        bf16x8 a = *(const bf16x8*)(sMem + l15 * MST + ks * 32 + g * 8);
        a1 = mfma16(a, w1[ks], a1);
      }
      if (g < 2) {
#pragma unroll
        for (int r = 0; r < 4; ++r) {
          int row = g * 4 + r;
          u16 vb = f2bf(a1[r]);
          sV[row * VST + wv * 16 + l15] = vb;
          vmem[((size_t)s * MM_ + row) * HE_ + h * E_ + wv * 16 + l15] = vb;
        }
      }
      if (tid < 320) {
        unsigned v = ld_gran(pg + ((size_t)par * 32 + h) * 320 + tid, tag);
        sP[(tid / 40) * PST + (tid % 40)] = (u16)v;
      }
      __syncthreads();
      {
        bf16x8 p0 = *(const bf16x8*)(sP + l15 * PST + g * 8);
        bf16x8 p1 = *(const bf16x8*)(sP + l15 * PST + 32 + g * 8);
        f32x4 o = (f32x4){0.f, 0.f, 0.f, 0.f};
        o = mfma16(p0, tr_frag(sV, VST, 0, wv * 16, lane), o);
        o = mfma16(p1, tr_frag(sV, VST, 32, wv * 16, lane), o);
        if (g < 2) {
#pragma unroll
          for (int r = 0; r < 4; ++r) sQ[(g * 4 + r) * 136 + wv * 16 + l15] = f2bf(o[r]);
        }
      }
      __syncthreads();
      {
        bf16x8 a[4];
#pragma unroll
        for (int ks = 0; ks < 4; ++ks) a[ks] = *(const bf16x8*)(sQ + l15 * 136 + ks * 32 + g * 8);
#pragma unroll
        for (int t = 0; t < 5; ++t) {
          int nt = wv + 8 * t;
          if (nt < 36) {
            f32x4 o = (f32x4){0.f, 0.f, 0.f, 0.f};
#pragma unroll
            for (int ks = 0; ks < 4; ++ks) o = mfma16(a[ks], w2[t * 4 + ks], o);
            if (g < 2) {
              int col = nt * 16 + l15, j = col / 18, cc = col % 18;
#pragma unroll
              for (int r = 0; r < 4; ++r)
                st_gran(partg + (((size_t)par * 32 + j) * 32 + h) * 144 + (g * 4 + r) * 18 + cc, tag, __float_as_uint(o[r]));
            }
          }
        }
      }
      {
        const u64* src = partg + (((size_t)par * 32 + h) * 32 + wv * 4) * 144;
#pragma unroll
        for (int i = 0; i < 3; ++i) {
          int e = lane + 64 * i;
          if (e < 144) {
            float a = 0.f;
#pragma unroll
            for (int q = 0; q < 4; ++q) a += __uint_as_float(ld_gran(src + q * 144 + e, tag));
            sRed[wv][e] = a;
          }
        }
      }
      __syncthreads();
      if (tid < 72 && s + 1 < NSEG_) {
        int r = tid / 9, pp = tid % 9;
        float x0 = 0.f, x1 = 0.f;
#pragma unroll
        for (int q = 0; q < 8; ++q) { x0 += sRed[q][r * 18 + pp * 2]; x1 += sRed[q][r * 18 + pp * 2 + 1]; }
        unsigned v = (unsigned)f2bf(x0) | ((unsigned)f2bf(x1) << 16);
        st_gran(memg + (size_t)(par ^ 1) * 2304 + h * 72 + tid, tag + 1, v);
      }
      __syncthreads();
    }
  }
}

extern "C" void kernel_launch(void* const* d_in, const int* in_sizes, int n_in, void* d_out, int out_size, void* d_ws, size_t ws_size,
                              hipStream_t stream) {
  const u16* x = (const u16*)d_in[0];
  const u16* mem0 = (const u16*)d_in[1];
  const u16* Wq = (const u16*)d_in[2];
  const u16* Wk = (const u16*)d_in[3];
  const u16* Wv = (const u16*)d_in[4];
  const u16* Wo = (const u16*)d_in[5];
  const u16* Wg = (const u16*)d_in[8];
  const u16* Wu = (const u16*)d_in[9];
  const u16* Wd = (const u16*)d_in[10];
  const u16* c = (const u16*)d_in[11];
  const u16* wmod = (const u16*)d_in[12];
  char* ws = (char*)d_ws;
  size_t off = 0;
  auto take = [&](size_t bytes) { char* p = ws + off; off += (bytes + 255) & ~(size_t)255; return p; };
  u64* memg = (u64*)take(2 * 2304 * 8);
  u64* pg = (u64*)take(2 * 32 * 320 * 8);
  u64* partg = (u64*)take((size_t)2 * 32 * 32 * 144 * 8);
  size_t zbytes = off;
  float* mod = (float*)take(6 * D_ * 4);
  u16* h1 = (u16*)take((size_t)S_ * D_ * 2);
  u16* qkv = (u16*)take((size_t)S_ * QKV_ * 2);
  u16* kmem = (u16*)take((size_t)NSEG_ * MM_ * HE_ * 2);
  u16* vmem = (u16*)take((size_t)NSEG_ * MM_ * HE_ * 2);
  u16* ox = (u16*)take((size_t)S_ * HE_ * 2);
  float* x1 = (float*)take((size_t)S_ * D_ * 4);
  u16* h2 = (u16*)take((size_t)S_ * D_ * 2);
  u16* tt = (u16*)take((size_t)S_ * F_ * 2);
  hipMemsetAsync(d_ws, 0, zbytes, stream);
  k_mod<<<54, 256, 0, stream>>>(c, wmod, mod);
  k_ln<false><<<S_ / 4, 256, 0, stream>>>(x, mod + 0 * D_, mod + 1 * D_, h1);
  k_gemm<0><<<dim3(96, 16), 256, 0, stream>>>(h1, D_, Wq, Wk, Wv, qkv, nullptr, nullptr);
  k_chain<<<64, 512, 0, stream>>>(mem0, Wq, Wk, Wv, Wo, qkv, kmem, vmem, memg, pg, partg);
  k_attn_x<<<NSEG_ * H_ / 4, 256, 0, stream>>>(qkv, kmem, vmem, ox);
  k_gemm<1><<<dim3(5, 16), 256, 0, stream>>>(ox, HE_, Wo, nullptr, nullptr, x1, x, mod + 2 * D_);
  k_ln<true><<<S_ / 4, 256, 0, stream>>>(x1, mod + 3 * D_, mod + 4 * D_, h2);
  k_gemm<2><<<dim3(72, 16), 256, 0, stream>>>(h2, D_, Wg, Wu, nullptr, tt, nullptr, nullptr);
  k_gemm<3><<<dim3(5, 16), 256, 0, stream>>>(tt, F_, Wd, nullptr, nullptr, d_out, x1, mod + 5 * D_);
}
```

```cpp
#include <hip/hip_runtime.h>
#include <hip/hip_bf16.h>
#include <stdint.h>

typedef unsigned short u16;
typedef short bf16x8 __attribute__((ext_vector_type(8)));
typedef short bf16x4 __attribute__((ext_vector_type(4)));
typedef float f32x4 __attribute__((ext_vector_type(4)));
typedef __attribute__((address_space(3))) bf16x4 lds_bf16x4;

constexpr int S_ = 2048, D_ = 576, H_ = 32, E_ = 128, HE_ = 4096, F_ = 4608, MM_ = 8, SEG_ = 32, NSEG_ = 64;
constexpr int QKV_ = 3 * HE_;

__device__ __forceinline__ float bf2f(u16 v) { return __uint_as_float(((unsigned)v) << 16); }
__device__ __forceinline__ u16 f2bf(float f) {
  unsigned u = __float_as_uint(f);
  u += 0x7fffu + ((u >> 16) & 1u);
  return (u16)(u >> 16);
}
__device__ __forceinline__ f32x4 mfma16(bf16x8 a, bf16x8 b, f32x4 c) {
  return __builtin_amdgcn_mfma_f32_16x16x32_bf16(a, b, c, 0, 0, 0);
}
__device__ __forceinline__ bf16x8 tr_frag(const u16* lds_tile, int stride, int k0, int n0, int lane) {
  int g = lane >> 4, q = (lane & 15) >> 2, p = lane & 3;
  const u16* a0 = lds_tile + (k0 + 8 * g + q) * stride + n0 + 4 * p;
  bf16x4 r0 = __builtin_amdgcn_ds_read_tr16_b64_v4i16((lds_bf16x4*)a0);
  bf16x4 r1 = __builtin_amdgcn_ds_read_tr16_b64_v4i16((lds_bf16x4*)(a0 + 4 * stride));
  bf16x8 r;
  r[0] = r0[0]; r[1] = r0[1]; r[2] = r0[2]; r[3] = r0[3];
  r[4] = r1[0]; r[5] = r1[1]; r[6] = r1[2]; r[7] = r1[3];
  return r;
}

__global__ __launch_bounds__(256) void k_mod(const u16* __restrict__ c, const u16* __restrict__ w, float* __restrict__ mod) {
  __shared__ float sc[D_];
  __shared__ float red[4][64];
  for (int i = threadIdx.x; i < D_; i += 256) {
    float v = bf2f(c[i]);
    float s = v / (1.f + __expf(-v));
    sc[i] = bf2f(f2bf(s));
  }
  __syncthreads();
  int n = blockIdx.x * 64 + (threadIdx.x & 63);
  int part = threadIdx.x >> 6;
  float acc = 0.f;
  for (int k = part * 144; k < part * 144 + 144; ++k) acc += sc[k] * bf2f(w[(size_t)k * (6 * D_) + n]);
  red[part][threadIdx.x & 63] = acc;
  __syncthreads();
  if (part == 0) {
    float a = red[0][threadIdx.x] + red[1][threadIdx.x] + red[2][threadIdx.x] + red[3][threadIdx.x];
    mod[n] = bf2f(f2bf(a));
  }
}

template <bool F32IN>
__global__ __launch_bounds__(256) void k_ln(const void* __restrict__ xin, const float* __restrict__ sh, const float* __restrict__ scl,
                                            u16* __restrict__ out) {
  int row = blockIdx.x * 4 + (threadIdx.x >> 6);
  int lane = threadIdx.x & 63;
  float v[9];
  float s = 0.f;
#pragma unroll
  for (int i = 0; i < 9; ++i) {
    int c = lane + 64 * i;
    v[i] = F32IN ? ((const float*)xin)[(size_t)row * D_ + c] : bf2f(((const u16*)xin)[(size_t)row * D_ + c]);
    s += v[i];
  }
#pragma unroll
  for (int o = 32; o > 0; o >>= 1) s += __shfl_xor(s, o);
  float m = s * (1.f / D_);
  float q = 0.f;
#pragma unroll
  for (int i = 0; i < 9; ++i) { float d = v[i] - m; q += d * d; }
#pragma unroll
  for (int o = 32; o > 0; o >>= 1) q += __shfl_xor(q, o);
  float rs = rsqrtf(q * (1.f / D_) + 1e-5f);
#pragma unroll
  for (int i = 0; i < 9; ++i) {
    int c = lane + 64 * i;
    out[(size_t)row * D_ + c] = f2bf((v[i] - m) * rs * (1.f + scl[c]) + sh[c]);
  }
}

constexpr int AST = 72, BST = 136;
template <int MODE>
__device__ __forceinline__ const u16* bsrc(const u16* Bp, const u16* B0, const u16* B1, int ldb, int n0, int r, int c) {
  if (MODE == 2) {
    int w = c >> 6, rr = c & 63;
    return (rr < 32 ? B0 : B1) + (size_t)r * ldb + n0 + 32 * w + (rr & 31);
  } else if (MODE == 0) {
    return Bp + (size_t)r * ldb + n0 + c;
  } else {
    int cc = n0 + c; if (cc > D_ - 8) cc = D_ - 8;
    return Bp + (size_t)r * ldb + cc;
  }
}
template <int MODE>
__global__ __launch_bounds__(256) void k_gemm(const u16* __restrict__ A, int K, const u16* __restrict__ B0, const u16* __restrict__ B1,
                                              const u16* __restrict__ B2, void* __restrict__ Cout, const void* __restrict__ res,
                                              const float* __restrict__ gvec) {
  __shared__ __attribute__((aligned(16))) u16 sA[128 * AST];
  __shared__ __attribute__((aligned(16))) u16 sB[64 * BST];
  const int tid = threadIdx.x, lane = tid & 63, wid = tid >> 6, wm = wid >> 1, wn = wid & 1;
  const int m0 = blockIdx.y * 128;
  const int nb = blockIdx.x;
  int ldb;
  const u16* Bp;
  int n0;
  if (MODE == 0) { int n = nb * 128; int which = n / HE_; Bp = which == 0 ? B0 : (which == 1 ? B1 : B2); n0 = n - which * HE_; ldb = HE_; }
  else if (MODE == 2) { n0 = nb * 64; ldb = F_; Bp = B0; }
  else { n0 = nb * 128; ldb = D_; Bp = B0; }
  f32x4 acc[4][4];
#pragma unroll
  for (int i = 0; i < 4; ++i)
#pragma unroll
    for (int j = 0; j < 4; ++j) acc[i][j] = (f32x4){0.f, 0.f, 0.f, 0.f};
  uint4 ra0, ra1, ra2, ra3, rb0, rb1, rb2, rb3;
#define GL_A(i, k0) (*(const uint4*)(A + (size_t)(m0 + ((tid + 256 * i) >> 3)) * K + (k0) + ((tid + 256 * i) & 7) * 8))
#define GL_B(i, k0) (*(const uint4*)bsrc<MODE>(Bp, B0, B1, ldb, n0, ((tid + 256 * i) >> 4) + (k0), ((tid + 256 * i) & 15) * 8))
#define gload(k0) do { ra0 = GL_A(0, k0); ra1 = GL_A(1, k0); ra2 = GL_A(2, k0); ra3 = GL_A(3, k0); \
    rb0 = GL_B(0, k0); rb1 = GL_B(1, k0); rb2 = GL_B(2, k0); rb3 = GL_B(3, k0); } while (0)
  gload(0);
  for (int k0 = 0; k0 < K; k0 += 64) {
#define ST_AB(i, va, vb) *(uint4*)(sA + ((tid + 256 * i) >> 3) * AST + ((tid + 256 * i) & 7) * 8) = va; \
    *(uint4*)(sB + ((tid + 256 * i) >> 4) * BST + ((tid + 256 * i) & 15) * 8) = vb;
    ST_AB(0, ra0, rb0) ST_AB(1, ra1, rb1) ST_AB(2, ra2, rb2) ST_AB(3, ra3, rb3)
    __syncthreads();
    if (k0 + 64 < K) gload(k0 + 64);
#pragma unroll
    for (int ks = 0; ks < 2; ++ks) {
      bf16x8 af[4], bfr[4];
#pragma unroll
      for (int i = 0; i < 4; ++i) af[i] = *(const bf16x8*)(sA + (wm * 64 + i * 16 + (lane & 15)) * AST + ks * 32 + (lane >> 4) * 8);
#pragma unroll
      for (int j = 0; j < 4; ++j) bfr[j] = tr_frag(sB, BST, ks * 32, wn * 64 + j * 16, lane);
#pragma unroll
      for (int i = 0; i < 4; ++i)
#pragma unroll
        for (int j = 0; j < 4; ++j) acc[i][j] = mfma16(af[i], bfr[j], acc[i][j]);
    }
    __syncthreads();
  }
#pragma unroll
  for (int i = 0; i < 4; ++i) {
#pragma unroll
    for (int r = 0; r < 4; ++r) {
      int row = m0 + wm * 64 + i * 16 + (lane >> 4) * 4 + r;
      if (MODE == 2) {
#pragma unroll
        for (int j = 0; j < 2; ++j) {
          int col = n0 + 32 * wn + j * 16 + (lane & 15);
          float g = bf2f(f2bf(acc[i][j][r])), u = bf2f(f2bf(acc[i][j + 2][r]));
          float t = g > 0.f ? g * u : 0.f;
          ((u16*)Cout)[(size_t)row * F_ + col] = f2bf(t);
        }
      } else {
#pragma unroll
        for (int j = 0; j < 4; ++j) {
          int col = nb * 128 + wn * 64 + j * 16 + (lane & 15);
          if (MODE == 0) {
            ((u16*)Cout)[(size_t)row * QKV_ + col] = f2bf(acc[i][j][r]);
          } else if (col < D_) {
            float a = bf2f(f2bf(acc[i][j][r]));
            if (MODE == 1) {
              float xv = bf2f(((const u16*)res)[(size_t)row * D_ + col]);
              ((float*)Cout)[(size_t)row * D_ + col] = xv + gvec[col] * a;
            } else {
              float xv = ((const float*)res)[(size_t)row * D_ + col];
              ((u16*)Cout)[(size_t)row * D_ + col] = f2bf(xv + gvec[col] * a);
            }
          }
        }
      }
    }
  }
}

constexpr int VST = 136;
constexpr int PST = 72;
__global__ __launch_bounds__(256) void k_attn_x(const u16* __restrict__ qkv, const u16* __restrict__ kmem, const u16* __restrict__ vmem,
                                                u16* __restrict__ ox) {
  __shared__ __attribute__((aligned(16))) u16 sV[4][64 * VST];
  __shared__ __attribute__((aligned(16))) u16 sP[4][32 * PST];
  const int lane = threadIdx.x & 63, wid = threadIdx.x >> 6;
  const int item = blockIdx.x * 4 + wid;
  const int s = item >> 5, h = item & 31;
  u16* V = sV[wid];
  u16* P = sP[wid];
  const u16* qbase = qkv + (size_t)(s * SEG_) * QKV_ + h * E_;
  const u16* kx = qbase + HE_;
  const u16* vx = qbase + 2 * HE_;
  const u16* km = kmem + ((size_t)s * MM_) * HE_ + h * E_;
  const u16* vm = vmem + ((size_t)s * MM_) * HE_ + h * E_;
#pragma unroll
  for (int i = 0; i < 16; ++i) {
    int ch = lane + 64 * i, r = ch >> 4, c = (ch & 15) * 8;
    uint4 v = make_uint4(0, 0, 0, 0);
    if (r < 8) v = *(const uint4*)(vm + (size_t)r * HE_ + c);
    else if (r < 40) v = *(const uint4*)(vx + (size_t)(r - 8) * QKV_ + c);
    *(uint4*)(V + r * VST + c) = v;
  }
  const int g = lane >> 4, l15 = lane & 15;
  f32x4 sc[2][3];
#pragma unroll
  for (int i = 0; i < 2; ++i)
#pragma unroll
    for (int j = 0; j < 3; ++j) sc[i][j] = (f32x4){0.f, 0.f, 0.f, 0.f};
#pragma unroll
  for (int ks = 0; ks < 4; ++ks) {
    bf16x8 a[2], b[3];
#pragma unroll
    for (int i = 0; i < 2; ++i) a[i] = *(const bf16x8*)(qbase + (size_t)(i * 16 + l15) * QKV_ + ks * 32 + g * 8);
    b[0] = *(const bf16x8*)(km + (size_t)(l15 & 7) * HE_ + ks * 32 + g * 8);
    b[1] = *(const bf16x8*)(kx + (size_t)l15 * QKV_ + ks * 32 + g * 8);
    b[2] = *(const bf16x8*)(kx + (size_t)(16 + l15) * QKV_ + ks * 32 + g * 8);
#pragma unroll
    for (int i = 0; i < 2; ++i)
#pragma unroll
      for (int j = 0; j < 3; ++j) sc[i][j] = mfma16(a[i], b[j], sc[i][j]);
  }
  const float scale = 0.08838834764831845f;
#pragma unroll
  for (int i = 0; i < 2; ++i) {
#pragma unroll
    for (int r = 0; r < 4; ++r) {
      int qi = i * 16 + g * 4 + r;
      float v0 = (l15 < 8) ? sc[i][0][r] * scale : -1e30f;
      float v1 = (l15 <= qi) ? sc[i][1][r] * scale : -1e30f;
      float v2 = (16 + l15 <= qi) ? sc[i][2][r] * scale : -1e30f;
      float mx = fmaxf(v0, fmaxf(v1, v2));
#pragma unroll
      for (int o = 1; o < 16; o <<= 1) mx = fmaxf(mx, __shfl_xor(mx, o));
      float e0 = (l15 < 8) ? __expf(v0 - mx) : 0.f;
      float e1 = (l15 <= qi) ? __expf(v1 - mx) : 0.f;
      float e2 = (16 + l15 <= qi) ? __expf(v2 - mx) : 0.f;
      float sm = e0 + e1 + e2;
#pragma unroll
      for (int o = 1; o < 16; o <<= 1) sm += __shfl_xor(sm, o);
      float inv = 1.f / sm;
      if (l15 < 8) P[qi * PST + l15] = f2bf(e0 * inv);
      else P[qi * PST + 32 + l15] = 0;
      P[qi * PST + 8 + l15] = f2bf(e1 * inv);
      P[qi * PST + 24 + l15] = f2bf(e2 * inv);
      P[qi * PST + 48 + l15] = 0;
    }
  }
  __syncthreads();
#pragma unroll
  for (int i = 0; i < 2; ++i) {
    bf16x8 a0 = *(const bf16x8*)(P + (i * 16 + l15) * PST + g * 8);
    bf16x8 a1 = *(const bf16x8*)(P + (i * 16 + l15) * PST + 32 + g * 8);
#pragma unroll
    for (int j = 0; j < 8; ++j) {
      f32x4 o = (f32x4){0.f, 0.f, 0.f, 0.f};
      o = mfma16(a0, tr_frag(V, VST, 0, j * 16, lane), o);
      o = mfma16(a1, tr_frag(V, VST, 32, j * 16, lane), o);
#pragma unroll
      for (int r = 0; r < 4; ++r)
        ox[(size_t)(s * SEG_ + i * 16 + g * 4 + r) * HE_ + h * E_ + j * 16 + l15] = f2bf(o[r]);
    }
  }
}

typedef unsigned long long u64;
#define SPIN_MAX (1 << 22)
__device__ __forceinline__ void st_gran(u64* p, unsigned tag, unsigned v) {
  __hip_atomic_store(p, ((u64)tag << 32) | v, __ATOMIC_RELAXED, __HIP_MEMORY_SCOPE_AGENT);
}
__device__ __forceinline__ unsigned ld_gran(const u64* p, unsigned tag) {
  for (int it = 0; it < SPIN_MAX; ++it) {
    u64 x = __hip_atomic_load(p, __ATOMIC_RELAXED, __HIP_MEMORY_SCOPE_AGENT);
    if ((unsigned)(x >> 32) == tag) return (unsigned)x;
    __builtin_amdgcn_s_sleep(1);
  }
  return 0u;
}
constexpr int MST = 584;
struct ChainBufs {
  u64* memg;
  u64* pg;
  u64* partg;
};

__global__ __launch_bounds__(512) void k_chain(const u16* __restrict__ mem0, const u16* __restrict__ Wq, const u16* __restrict__ Wk,
                                               const u16* __restrict__ Wv, const u16* __restrict__ Wo, const u16* __restrict__ qkv,
                                               u16* __restrict__ kmem, u16* __restrict__ vmem, u64* memg, u64* pg, u64* partg) {
  __shared__ __attribute__((aligned(16))) u16 sMem[16 * MST];
  __shared__ __attribute__((aligned(16))) u16 sQ[16 * 136];
  __shared__ __attribute__((aligned(16))) u16 sK[16 * 136];
  __shared__ __attribute__((aligned(16))) u16 sV[64 * VST];
  __shared__ __attribute__((aligned(16))) u16 sP[16 * PST];
  __shared__ __attribute__((aligned(16))) u16 sStage[32 * MST];
  __shared__ __attribute__((aligned(16))) u16 sWo5[4 * 8 * 64 * 8];
  __shared__ float sS[8 * 48];
  __shared__ float sRed[8][144];
  const int tid = threadIdx.x, lane = tid & 63, wv = tid >> 6;
  const int g = lane >> 4, l15 = lane & 15;
  const int h = blockIdx.x >> 1;
  const bool roleA = (blockIdx.x & 1) == 0;
  for (int i = tid; i < 16 * MST; i += 512) sMem[i] = 0;
  for (int i = tid; i < 16 * 136; i += 512) { sQ[i] = 0; sK[i] = 0; }
  for (int i = tid; i < 64 * VST; i += 512) sV[i] = 0;
  for (int i = tid; i < 16 * PST; i += 512) sP[i] = 0;
  bf16x8 w1[18], w2[18];
  {
    u16* stg = sStage;
    const u16* Wa = roleA ? Wq : Wv;
#pragma unroll
    for (int ks = 0; ks < 18; ++ks) {
      __syncthreads();
      { int r = tid >> 4, c = (tid & 15) * 8;
        *(uint4*)(stg + r * 136 + c) = *(const uint4*)(Wa + (size_t)(ks * 32 + r) * HE_ + h * E_ + c); }
      __syncthreads();
      w1[ks] = tr_frag(stg, 136, 0, wv * 16, lane);
    }
    if (roleA) {
#pragma unroll
      for (int ks = 0; ks < 18; ++ks) {
        __syncthreads();
        { int r = tid >> 4, c = (tid & 15) * 8;
          *(uint4*)(stg + r * 136 + c) = *(const uint4*)(Wk + (size_t)(ks * 32 + r) * HE_ + h * E_ + c); }
        __syncthreads();
        w2[ks] = tr_frag(stg, 136, 0, wv * 16, lane);
      }
    } else {
#pragma unroll
      for (int ks = 0; ks < 4; ++ks) {
        __syncthreads();
        for (int i = tid; i < 32 * 72; i += 512) {
          int r = i / 72, c = (i % 72) * 8;
          *(uint4*)(stg + r * MST + c) = *(const uint4*)(Wo + (size_t)(h * E_ + ks * 32 + r) * D_ + c);
        }
        __syncthreads();
#pragma unroll
        for (int t = 0; t < 5; ++t) {
          int nt = wv + 8 * t; if (nt > 35) nt = 35;
          bf16x8 f = tr_frag(stg, MST, 0, nt * 16, lane);
          if (t < 4) w2[t * 4 + ks] = f; else *(bf16x8*)(sWo5 + ((ks * 8 + wv) * 64 + lane) * 8) = f;
        }
      }
    }
  }
  __syncthreads();
  for (int s = 0; s < NSEG_; ++s) {
    const unsigned tag = s + 1;
    const int par = s & 1;
    const u16* xb = qkv + (size_t)(s * SEG_) * QKV_ + h * E_ + (roleA ? HE_ : 2 * HE_);
    {
      int r = tid >> 4, c = (tid & 15) * 8;
      uint4 v = *(const uint4*)(xb + (size_t)r * QKV_ + c);
      *(uint4*)(sV + (8 + r) * VST + c) = v;
    }
    if (s == 0) {
      for (int i = tid; i < 8 * 288; i += 512) {
        int r = i / 288, c = (i % 288) * 2;
        *(unsigned*)(sMem + r * MST + c) = *(const unsigned*)(mem0 + r * D_ + c);
      }
    } else {
      const u64* mg = memg + (size_t)par * 2304;
      u64 xv[5];
      for (int it = 0; it < SPIN_MAX; ++it) {
        bool ok = true;
#pragma unroll
        for (int q = 0; q < 5; ++q) {
          int i = tid + 512 * q; if (i >= 2304) i = tid;
          xv[q] = __hip_atomic_load(mg + i, __ATOMIC_RELAXED, __HIP_MEMORY_SCOPE_AGENT);
        }
#pragma unroll
        for (int q = 0; q < 5; ++q) ok &= (unsigned)(xv[q] >> 32) == tag;
        if (ok) break;
        __builtin_amdgcn_s_sleep(1);
      }
#pragma unroll
      for (int q = 0; q < 5; ++q) {
        int i = tid + 512 * q;
        if (i < 2304) {
          int j = i / 72, rem = i % 72, r = rem / 9, pp = rem % 9;
          *(unsigned*)(sMem + r * MST + j * 18 + pp * 2) = (unsigned)xv[q];
        }
      }
    }
    __syncthreads();
    f32x4 a1 = (f32x4){0.f, 0.f, 0.f, 0.f}, a2 = (f32x4){0.f, 0.f, 0.f, 0.f};
    if (roleA) {
#pragma unroll
      for (int ks = 0; ks < 18; ++ks) {
        bf16x8 a = *(const bf16x8*)(sMem + l15 * MST + ks * 32 + g * 8);
        a1 = mfma16(a, w1[ks], a1);
        a2 = mfma16(a, w2[ks], a2);
      }
      if (g < 2) {
#pragma unroll
        for (int r = 0; r < 4; ++r) {
          int row = g * 4 + r;
          sQ[row * 136 + wv * 16 + l15] = f2bf(a1[r]);
          u16 kb = f2bf(a2[r]);
          sK[row * 136 + wv * 16 + l15] = kb;
          kmem[((size_t)s * MM_ + row) * HE_ + h * E_ + wv * 16 + l15] = kb;
        }
      }
      __syncthreads();
      if (wv < 3) {
        f32x4 sc = (f32x4){0.f, 0.f, 0.f, 0.f};
        const u16* kb = wv == 0 ? (sK + l15 * 136) : (sV + (8 + (wv - 1) * 16 + l15) * VST);
#pragma unroll
        for (int ks = 0; ks < 4; ++ks) {
          bf16x8 a = *(const bf16x8*)(sQ + l15 * 136 + ks * 32 + g * 8);
          bf16x8 b = *(const bf16x8*)(kb + ks * 32 + g * 8);
          sc = mfma16(a, b, sc);
        }
        if (g < 2) {
#pragma unroll
          for (int r = 0; r < 4; ++r) {
            int col = wv == 0 ? l15 : 8 + (wv - 1) * 16 + l15;
            if (wv != 0 || l15 < 8) sS[(g * 4 + r) * 48 + col] = sc[r] * 0.08838834764831845f;
          }
        }
      }
      __syncthreads();
      if (wv == 0) {
        int row = lane >> 3, c0 = (lane & 7) * 5;
        float v[5], mx = -1e30f;
#pragma unroll
        for (int i = 0; i < 5; ++i) { v[i] = sS[row * 48 + c0 + i]; mx = fmaxf(mx, v[i]); }
#pragma unroll
        for (int o = 1; o < 8; o <<= 1) mx = fmaxf(mx, __shfl_xor(mx, o));
        float sm = 0.f;
#pragma unroll
        for (int i = 0; i < 5; ++i) { v[i] = __expf(v[i] - mx); sm += v[i]; }
#pragma unroll
        for (int o = 1; o < 8; o <<= 1) sm += __shfl_xor(sm, o);
        float inv = 1.f / sm;
        u64* dst = pg + ((size_t)par * 32 + h) * 320 + row * 40 + c0;
#pragma unroll
        for (int i = 0; i < 5; ++i) st_gran(dst + i, tag, (unsigned)f2bf(v[i] * inv));
      }
      __syncthreads();
    } else {
#pragma unroll
      for (int ks = 0; ks < 18; ++ks) {
        bf16x8 a = *(const bf16x8*)(sMem + l15 * MST + ks * 32 + g * 8);
        a1 = mfma16(a, w1[ks], a1);
      }
      if (g < 2) {
#pragma unroll
        for (int r = 0; r < 4; ++r) {
          int row = g * 4 + r;
          u16 vb = f2bf(a1[r]);
          sV[row * VST + wv * 16 + l15] = vb;
          vmem[((size_t)s * MM_ + row) * HE_ + h * E_ + wv * 16 + l15] = vb;
        }
      }
      if (tid < 320) {
        unsigned v = ld_gran(pg + ((size_t)par * 32 + h) * 320 + tid, tag);
        sP[(tid / 40) * PST + (tid % 40)] = (u16)v;
      }
      __syncthreads();
      {
        bf16x8 p0 = *(const bf16x8*)(sP + l15 * PST + g * 8);
        bf16x8 p1 = *(const bf16x8*)(sP + l15 * PST + 32 + g * 8);
        f32x4 o = (f32x4){0.f, 0.f, 0.f, 0.f};
        o = mfma16(p0, tr_frag(sV, VST, 0, wv * 16, lane), o);
        o = mfma16(p1, tr_frag(sV, VST, 32, wv * 16, lane), o);
        if (g < 2) {
#pragma unroll
          for (int r = 0; r < 4; ++r) sQ[(g * 4 + r) * 136 + wv * 16 + l15] = f2bf(o[r]);
        }
      }
      __syncthreads();
      {
        bf16x8 a[4];
#pragma unroll
        for (int ks = 0; ks < 4; ++ks) a[ks] = *(const bf16x8*)(sQ + l15 * 136 + ks * 32 + g * 8);
#pragma unroll
        for (int t = 0; t < 5; ++t) {
          int nt = wv + 8 * t;
          if (nt < 36) {
            f32x4 o = (f32x4){0.f, 0.f, 0.f, 0.f};
#pragma unroll
            for (int ks = 0; ks < 4; ++ks)
              o = mfma16(a[ks], t < 4 ? w2[(t & 3) * 4 + ks] : *(const bf16x8*)(sWo5 + ((ks * 8 + wv) * 64 + lane) * 8), o);
            if (g < 2) {
              int col = nt * 16 + l15, j = col / 18, cc = col % 18;
#pragma unroll
              for (int r = 0; r < 4; ++r)
                st_gran(partg + (((size_t)par * 32 + j) * 32 + h) * 144 + (g * 4 + r) * 18 + cc, tag, __float_as_uint(o[r]));
            }
          }
        }
      }
      {
        const u64* src = partg + (((size_t)par * 32 + h) * 32 + wv * 4) * 144;
        unsigned xv[12];
        for (int it = 0; it < SPIN_MAX; ++it) {
          bool ok = true;
#pragma unroll
          for (int i = 0; i < 3; ++i) {
            int e = lane + 64 * i; if (e >= 144) e = lane;
#pragma unroll
            for (int q = 0; q < 4; ++q) {
              u64 y = __hip_atomic_load(src + q * 144 + e, __ATOMIC_RELAXED, __HIP_MEMORY_SCOPE_AGENT);
              xv[i * 4 + q] = (unsigned)y;
              ok &= (unsigned)(y >> 32) == tag;
            }
          }
          if (ok) break;
          __builtin_amdgcn_s_sleep(1);
        }
#pragma unroll
        for (int i = 0; i < 3; ++i) {
          int e = lane + 64 * i;
          if (e < 144) {
            float a = 0.f;
#pragma unroll
            for (int q = 0; q < 4; ++q) a += __uint_as_float(xv[i * 4 + q]);
            sRed[wv][e] = a;
          }
        }
      }
      __syncthreads();
      if (tid < 72 && s + 1 < NSEG_) {
        int r = tid / 9, pp = tid % 9;
        float x0 = 0.f, x1 = 0.f;
#pragma unroll
        for (int q = 0; q < 8; ++q) { x0 += sRed[q][r * 18 + pp * 2]; x1 += sRed[q][r * 18 + pp * 2 + 1]; }
        unsigned v = (unsigned)f2bf(x0) | ((unsigned)f2bf(x1) << 16);
        st_gran(memg + (size_t)(par ^ 1) * 2304 + h * 72 + tid, tag + 1, v);
      }
      __syncthreads();
    }
  }
}

extern "C" void kernel_launch(void* const* d_in, const int* in_sizes, int n_in, void* d_out, int out_size, void* d_ws, size_t ws_size,
                              hipStream_t stream) {
  const u16* x = (const u16*)d_in[0];
  const u16* mem0 = (const u16*)d_in[1];
  const u16* Wq = (const u16*)d_in[2];
  const u16* Wk = (const u16*)d_in[3];
  const u16* Wv = (const u16*)d_in[4];
  const u16* Wo = (const u16*)d_in[5];
  const u16* Wg = (const u16*)d_in[8];
  const u16* Wu = (const u16*)d_in[9];
  const u16* Wd = (const u16*)d_in[10];
  const u16* c = (const u16*)d_in[11];
  const u16* wmod = (const u16*)d_in[12];
  char* ws = (char*)d_ws;
  size_t off = 0;
  auto take = [&](size_t bytes) { char* p = ws + off; off += (bytes + 255) & ~(size_t)255; return p; };
  u64* memg = (u64*)take(2 * 2304 * 8);
  u64* pg = (u64*)take(2 * 32 * 320 * 8);
  u64* partg = (u64*)take((size_t)2 * 32 * 32 * 144 * 8);
  size_t zbytes = off;
  float* mod = (float*)take(6 * D_ * 4);
  u16* h1 = (u16*)take((size_t)S_ * D_ * 2);
  u16* qkv = (u16*)take((size_t)S_ * QKV_ * 2);
  u16* kmem = (u16*)take((size_t)NSEG_ * MM_ * HE_ * 2);
  u16* vmem = (u16*)take((size_t)NSEG_ * MM_ * HE_ * 2);
  u16* ox = (u16*)take((size_t)S_ * HE_ * 2);
  float* x1 = (float*)take((size_t)S_ * D_ * 4);
  u16* h2 = (u16*)take((size_t)S_ * D_ * 2);
  u16* tt = (u16*)take((size_t)S_ * F_ * 2);
  hipMemsetAsync(d_ws, 0, zbytes, stream);
  k_mod<<<54, 256, 0, stream>>>(c, wmod, mod);
  k_ln<false><<<S_ / 4, 256, 0, stream>>>(x, mod + 0 * D_, mod + 1 * D_, h1);
  k_gemm<0><<<dim3(96, 16), 256, 0, stream>>>(h1, D_, Wq, Wk, Wv, qkv, nullptr, nullptr);
  k_chain<<<64, 512, 0, stream>>>(mem0, Wq, Wk, Wv, Wo, qkv, kmem, vmem, memg, pg, partg);
  k_attn_x<<<NSEG_ * H_ / 4, 256, 0, stream>>>(qkv, kmem, vmem, ox);
  k_gemm<1><<<dim3(5, 16), 256, 0, stream>>>(ox, HE_, Wo, nullptr, nullptr, x1, x, mod + 2 * D_);
  k_ln<true><<<S_ / 4, 256, 0, stream>>>(x1, mod + 3 * D_, mod + 4 * D_, h2);
  k_gemm<2><<<dim3(72, 16), 256, 0, stream>>>(h2, D_, Wg, Wu, nullptr, tt, nullptr, nullptr);
  k_gemm<3><<<dim3(5, 16), 256, 0, stream>>>(tt, F_, Wd, nullptr, nullptr, d_out, x1, mod + 5 * D_);
}
```
